```python
import torch

def _softmax(x, axis=-1):
    return torch.softmax(x, dim=axis)
def _gelu(x):
    return torch.nn.functional.gelu(x, approximate="tanh")
def _silu(x):
    return torch.nn.functional.silu(x)
def _sigmoid(x):
    return torch.sigmoid(x)
def _layernorm(x, axis=-1, eps=1e-5):
    m = x.mean(dim=axis, keepdim=True)
    v = x.var(dim=axis, keepdim=True, unbiased=False)
    return (x - m) / torch.sqrt(v + eps)
def _scatter_add(tgt, idx, src, axis=0):
    return tgt.clone().index_add_(axis, idx, src)
def _segment_sum(src, seg, n):
    out = torch.zeros((n,) + src.shape[1:], dtype=src.dtype)
    return out.index_add_(0, seg, src)
def _pad(x, pw):
    flat = tuple(v for pair in reversed(pw) for v in pair)
    return torch.nn.functional.pad(x, flat)

kernel_name = 'standalone__low_rank_proj_linformer__sampling'

def setup_inputs():

    _w = (lambda a: (lambda t: t.to(torch.bfloat16) if t.is_floating_point() else t)(torch.as_tensor(a)))
    B, H, Sq, Skv, Dh, R = 8, 32, 16, 4096, 192, 8
    return {
        "q": _w((lambda *s: torch.randn(*s, dtype=torch.bfloat16) * 0.8)(B, H, Sq, Dh)),
        "k": _w((lambda *s: torch.randn(*s, dtype=torch.bfloat16) * 0.8)(B, H, Skv, Dh)),
        "v": _w((lambda *s: torch.randn(*s, dtype=torch.bfloat16) * 0.05)(B, H, Skv, Dh)),
        "e_proj": _w((lambda *s: torch.randn(*s, dtype=torch.bfloat16) * 0.8)(R, Skv) * (Skv ** -0.5)),
        "f_proj": _w((lambda *s: torch.randn(*s, dtype=torch.bfloat16) * 0.05)(R, Skv) * (Skv ** -0.5)),
    }

def reference(q, k, v, e_proj, f_proj):
    B, H, Sq, Dh = q.shape
    kp = torch.einsum("rn,bhnd->bhrd", e_proj, k)
    vp = torch.einsum("rn,bhnd->bhrd", f_proj, v)
    scores = torch.einsum("bhqd,bhrd->bhqr", q, kp) * (Dh ** -0.5)
    p = _softmax(scores, axis=-1)
    o = torch.einsum("bhqr,bhrd->bhqd", p, vp)
    return o
```

```cpp
#include <hip/hip_runtime.h>
#include <hip/hip_bf16.h>
#include <stdint.h>

typedef unsigned u32x4 __attribute__((ext_vector_type(4)));
typedef unsigned u32x2 __attribute__((ext_vector_type(2)));
typedef float f32x4 __attribute__((ext_vector_type(4)));
typedef short s16x8 __attribute__((ext_vector_type(8)));
typedef short s16x4 __attribute__((ext_vector_type(4)));
#define GAS __attribute__((address_space(1)))

constexpr int NBH = 256, SQ = 16, SKV = 4096, DH = 192, RK = 8;
constexpr int ROWB = DH * 2;
constexpr int BLKROWS = 64;
constexpr int BLKB = BLKROWS * ROWB;

#define INTERLEAVE 0

__device__ __forceinline__ f32x4 mfma32(u32x4 a, u32x4 b, f32x4 c) {
  return __builtin_amdgcn_mfma_f32_16x16x32_bf16(__builtin_bit_cast(s16x8, a), __builtin_bit_cast(s16x8, b), c, 0, 0, 0);
}
__device__ __forceinline__ f32x4 mfma16(u32x2 a, u32x2 b, f32x4 c) {
  return __builtin_amdgcn_mfma_f32_16x16x16bf16_1k(__builtin_bit_cast(s16x4, a), __builtin_bit_cast(s16x4, b), c, 0, 0, 0);
}
__device__ __forceinline__ float ror8(float x) {
  return __builtin_bit_cast(float, __builtin_amdgcn_mov_dpp(__builtin_bit_cast(int, x), 0x128, 0xf, 0xf, true));
}
__device__ __forceinline__ unsigned pack_bf16(float lo, float hi) {
  typedef __bf16 bf2 __attribute__((ext_vector_type(2)));
  bf2 r; r[0] = (__bf16)lo; r[1] = (__bf16)hi;
  return __builtin_bit_cast(unsigned, r);
}
__device__ __forceinline__ float round_bf16(float x) { return (float)(__bf16)x; }

constexpr unsigned SEL_LO = 0x05040100u, SEL_HI = 0x07060302u;

__global__ void __launch_bounds__(512, 2)
linformer_kernel(const unsigned char* __restrict__ q, const unsigned char* __restrict__ k, const unsigned char* __restrict__ v,
                 const unsigned char* __restrict__ e, const unsigned char* __restrict__ f, unsigned char* __restrict__ out) {
  __shared__ __attribute__((aligned(16))) unsigned char lds[49152 + 6144 + 3072];
  float* red = (float*)lds;
  unsigned char* qs = lds + 49152;
  unsigned char* kps = lds + 49152 + 6144;

  const int bh = blockIdx.x;
  const int tid = threadIdx.x, lane = tid & 63, w = __builtin_amdgcn_readfirstlane(tid >> 6);
  const int g = lane >> 4, c = lane & 15;
  const int tensor = w >> 2, part = w & 3;
  const unsigned char* kv = (tensor ? v : k) + (size_t)bh * (SKV * ROWB);
  const unsigned char* ef = tensor ? f : e;
  const unsigned loff = 6144u * g + 16u * c;
  const unsigned eoff = 8192u * (c & 7) + 32u * g;
  const unsigned selB = (c >= 8) ? SEL_HI : SEL_LO;

  u32x4 qreg = {0u, 0u, 0u, 0u};
  if (tid < 384) qreg = *(const GAS u32x4*)(q + (size_t)bh * (SQ * ROWB) + tid * 16);

  f32x4 acc[3][8];
#pragma unroll
  for (int L = 0; L < 3; ++L)
#pragma unroll
    for (int j = 0; j < 8; ++j) acc[L][j] = f32x4{0.f, 0.f, 0.f, 0.f};

  if (tid < 384) *(u32x4*)(qs + tid * 16) = qreg;

  for (int blk = 0; blk < 16; ++blk) {
    const int nb = INTERLEAVE ? (blk * 4 + part) : (part * 16 + blk);
    const unsigned char* pe = ef + nb * (BLKROWS * 2) + eoff;
    const unsigned char* pd = kv + (size_t)nb * BLKB + loff;
    const u32x4 e0 = *(const GAS u32x4*)(pe);
    const u32x4 e1 = *(const GAS u32x4*)(pe + 16);
    u32x4 d[8][3];
#pragma unroll
    for (int i = 0; i < 8; ++i)
#pragma unroll
      for (int L = 0; L < 3; ++L) d[i][L] = __builtin_nontemporal_load((const GAS u32x4*)(pd + 768 * i + 256 * L));
    __builtin_amdgcn_sched_barrier(0);
    u32x4 A[3];
    A[0] = u32x4{__builtin_amdgcn_perm(e0[1], e0[0], SEL_LO), __builtin_amdgcn_perm(e0[3], e0[2], SEL_LO),
                 __builtin_amdgcn_perm(e1[1], e1[0], SEL_LO), __builtin_amdgcn_perm(e1[3], e1[2], SEL_LO)};
    A[1] = u32x4{__builtin_amdgcn_perm(e0[1], e0[0], selB), __builtin_amdgcn_perm(e0[3], e0[2], selB),
                 __builtin_amdgcn_perm(e1[1], e1[0], selB), __builtin_amdgcn_perm(e1[3], e1[2], selB)};
    A[2] = u32x4{__builtin_amdgcn_perm(e0[1], e0[0], SEL_HI), __builtin_amdgcn_perm(e0[3], e0[2], SEL_HI),
                 __builtin_amdgcn_perm(e1[1], e1[0], SEL_HI), __builtin_amdgcn_perm(e1[3], e1[2], SEL_HI)};
#pragma unroll
    for (int L = 0; L < 3; ++L)
#pragma unroll
      for (int wd = 0; wd < 4; ++wd) {
        const u32x4 blo = {__builtin_amdgcn_perm(d[1][L][wd], d[0][L][wd], SEL_LO), __builtin_amdgcn_perm(d[3][L][wd], d[2][L][wd], SEL_LO),
                           __builtin_amdgcn_perm(d[5][L][wd], d[4][L][wd], SEL_LO), __builtin_amdgcn_perm(d[7][L][wd], d[6][L][wd], SEL_LO)};
        const u32x4 bhi = {__builtin_amdgcn_perm(d[1][L][wd], d[0][L][wd], SEL_HI), __builtin_amdgcn_perm(d[3][L][wd], d[2][L][wd], SEL_HI),
                           __builtin_amdgcn_perm(d[5][L][wd], d[4][L][wd], SEL_HI), __builtin_amdgcn_perm(d[7][L][wd], d[6][L][wd], SEL_HI)};
        acc[L][2 * wd] = mfma32(A[L], blo, acc[L][2 * wd]);
        acc[L][2 * wd + 1] = mfma32(A[L], bhi, acc[L][2 * wd + 1]);
      }
    __builtin_amdgcn_sched_barrier(0);
  }

  const bool lo32 = lane < 32;
  const bool useB = lo32 && (c < 8);
  f32x4 R[8];
#pragma unroll
  for (int j = 0; j < 8; ++j)
#pragma unroll
    for (int t = 0; t < 4; ++t) {
      const float s1 = useB ? acc[1][j][t] : acc[0][j][t];
      const float s2 = lo32 ? acc[2][j][t] : acc[1][j][t];
      R[j][t] = s1 + ror8(s2);
    }
  const int dbase = useB ? (128 + 8 * c) : (8 * c);
  const bool active = lo32 || (c < 8);
  float* myred = red + w * 1536;
  if (active) {
    if (tensor == 0) {
#pragma unroll
      for (int t = 0; t < 4; ++t) {
        float* p = myred + (4 * (g & 1) + t) * DH + dbase;
        *(f32x4*)(p) = f32x4{R[0][t], R[1][t], R[2][t], R[3][t]};
        *(f32x4*)(p + 4) = f32x4{R[4][t], R[5][t], R[6][t], R[7][t]};
      }
    } else {
#pragma unroll
      for (int j = 0; j < 8; ++j) *(f32x4*)(myred + (dbase + j) * RK + 4 * (g & 1)) = R[j];
    }
  }
  __syncthreads();
  if (tid < 384) {
    f32x4 s = *(const f32x4*)(red + 0 * 1536 + tid * 4);
    s += *(const f32x4*)(red + 1 * 1536 + tid * 4);
    s += *(const f32x4*)(red + 2 * 1536 + tid * 4);
    s += *(const f32x4*)(red + 3 * 1536 + tid * 4);
    *(u32x2*)(kps + tid * 8) = u32x2{pack_bf16(s[0], s[1]), pack_bf16(s[2], s[3])};
  }
  __syncthreads();
  if (w < 6) {
    f32x4 st = {0.f, 0.f, 0.f, 0.f};
#pragma unroll
    for (int s = 0; s < 6; ++s) {
      const u32x4 a = *(const u32x4*)(kps + (c & 7) * ROWB + 64 * s + 16 * g);
      const u32x4 b = *(const u32x4*)(qs + c * ROWB + 64 * s + 16 * g);
      st = mfma32(a, b, st);
    }
    float sc[4];
#pragma unroll
    for (int t = 0; t < 4; ++t) sc[t] = round_bf16(round_bf16(st[t]) * 0.07216878364870322f);
    float m = fmaxf(fmaxf(sc[0], sc[1]), fmaxf(sc[2], sc[3]));
    m = fmaxf(m, __shfl_xor(m, 16));
    float ex[4], sum = 0.f;
#pragma unroll
    for (int t = 0; t < 4; ++t) { ex[t] = __expf(sc[t] - m); sum += ex[t]; }
    sum += __shfl_xor(sum, 16);
    const float inv = 1.0f / sum;
    u32x2 pb = {pack_bf16(ex[0] * inv, ex[1] * inv), pack_bf16(ex[2] * inv, ex[3] * inv)};
    if (!lo32) pb = u32x2{0u, 0u};
#pragma unroll
    for (int u = 0; u < 2; ++u) {
      const int dt = 2 * w + u;
      const float* pv = red + (4 + 2 * (g >> 1)) * 1536 + (16 * dt + c) * RK + 4 * (g & 1);
      f32x4 s = *(const f32x4*)(pv) + *(const f32x4*)(pv + 1536);
      f32x4 o;
#pragma unroll
      for (int t = 0; t < 4; ++t) o[t] = __shfl_xor(s[t], 32);
      f32x4 tot = lo32 ? (s + o) : (o + s);
      u32x2 a = {pack_bf16(tot[0], tot[1]), pack_bf16(tot[2], tot[3])};
      if (!lo32) a = u32x2{0u, 0u};
      const f32x4 ot = mfma16(a, pb, f32x4{0.f, 0.f, 0.f, 0.f});
      *(GAS u32x2*)(out + (size_t)bh * (SQ * ROWB) + c * ROWB + (16 * dt + 4 * g) * 2) = u32x2{pack_bf16(ot[0], ot[1]), pack_bf16(ot[2], ot[3])};
    }
  }
}

extern "C" void kernel_launch(void* const* d_in, const int* in_sizes, int n_in, void* d_out, int out_size, void* d_ws, size_t ws_size,
                              hipStream_t stream) {
  (void)in_sizes; (void)n_in; (void)out_size; (void)d_ws; (void)ws_size;
  linformer_kernel<<<NBH, 512, 0, stream>>>((const unsigned char*)d_in[0], (const unsigned char*)d_in[1], (const unsigned char*)d_in[2],
                                            (const unsigned char*)d_in[3], (const unsigned char*)d_in[4], (unsigned char*)d_out);
}
```

```cpp
#include <hip/hip_runtime.h>
#include <hip/hip_bf16.h>
#include <stdint.h>

typedef unsigned u32x4 __attribute__((ext_vector_type(4)));
typedef unsigned u32x2 __attribute__((ext_vector_type(2)));
typedef float f32x4 __attribute__((ext_vector_type(4)));
typedef short s16x8 __attribute__((ext_vector_type(8)));
typedef short s16x4 __attribute__((ext_vector_type(4)));
#define GAS __attribute__((address_space(1)))
#define LAS __attribute__((address_space(3)))

constexpr int NBH = 256, SQ = 16, SKV = 4096, DH = 192;
constexpr int ROWB = DH * 2;
constexpr int BLKROWS = 64;
constexpr int BLKB = BLKROWS * ROWB;

#define STAMP 0
#define PACE_T 1165
#define PACE_N0 72
#define PACE_G 8
#define DITHER 1
#define TOKEN 1
#define TOK0 3
#define NOE 0
constexpr unsigned PACE_TK = (unsigned)((unsigned long long)PACE_T * 10ull * 65536ull / 3072ull);

__device__ __forceinline__ f32x4 mfma32(u32x4 a, u32x4 b, f32x4 c) {
  return __builtin_amdgcn_mfma_f32_16x16x32_bf16(__builtin_bit_cast(s16x8, a), __builtin_bit_cast(s16x8, b), c, 0, 0, 0);
}
__device__ __forceinline__ f32x4 mfma16(u32x2 a, u32x2 b, f32x4 c) {
  return __builtin_amdgcn_mfma_f32_16x16x16bf16_1k(__builtin_bit_cast(s16x4, a), __builtin_bit_cast(s16x4, b), c, 0, 0, 0);
}
__device__ __forceinline__ unsigned ror8u(unsigned x) { return (unsigned)__builtin_amdgcn_mov_dpp((int)x, 0x128, 0xf, 0xf, true); }
__device__ __forceinline__ float ror8(float x) { return __builtin_bit_cast(float, ror8u(__builtin_bit_cast(unsigned, x))); }
__device__ __forceinline__ unsigned pack_bf16(float lo, float hi) {
  typedef __bf16 bf2 __attribute__((ext_vector_type(2)));
  bf2 r; r[0] = (__bf16)lo; r[1] = (__bf16)hi;
  return __builtin_bit_cast(unsigned, r);
}
__device__ __forceinline__ float round_bf16(float x) { return (float)(__bf16)x; }
__device__ __forceinline__ unsigned now32() { return (unsigned)__builtin_amdgcn_s_memrealtime(); }

constexpr unsigned SEL_LO = 0x05040100u, SEL_HI = 0x07060302u;

__device__ __forceinline__ void pace(unsigned tbase, int kib) {
  if (PACE_T > 0 && kib > 0) {
    const unsigned dl = tbase + (((unsigned)kib * PACE_TK) >> 16);
    for (;;) {
      const int rem = (int)(dl - now32());
      if (rem <= 0) break;
      if (rem > 60) __builtin_amdgcn_s_sleep(16); else __builtin_amdgcn_s_sleep(1);
    }
  }
}

struct Blk { u32x4 d[24]; u32x4 e; };

__device__ __forceinline__ void issue(Blk& b, const unsigned char* __restrict__ x, const unsigned char* __restrict__ pr, int nb, int m, unsigned loff,
                                      unsigned eoff, unsigned tbase, volatile LAS int* tok) {
  const unsigned char* pe = pr + (NOE ? 0 : nb) * (BLKROWS * 2) + eoff;
  const unsigned char* pd = x + (size_t)nb * BLKB + loff;
#pragma unroll
  for (int s = 0; s < 24 / PACE_G; ++s) {
    pace(tbase, m * 24 + s * PACE_G - PACE_N0);
    if (TOKEN && s == 0 && m >= TOK0) { while (*tok != m) __builtin_amdgcn_s_sleep(1); }
    __builtin_amdgcn_sched_barrier(0);
#pragma unroll
    for (int i = s * PACE_G; i < (s + 1) * PACE_G; ++i) b.d[i] = __builtin_nontemporal_load((const GAS u32x4*)(pd + 256 * i));
    if (s == 0) b.e = *(const GAS u32x4*)(pe);
    __builtin_amdgcn_sched_barrier(0);
  }
  if (TOKEN && m >= TOK0) *tok = m + 1;
  __builtin_amdgcn_sched_barrier(0);
}

__device__ __forceinline__ void compute(const Blk& b, f32x4 (&acc)[3][8], bool hi8) {
  const unsigned selB = hi8 ? SEL_HI : SEL_LO;
  u32x4 e0, e1;
#pragma unroll
  for (int i = 0; i < 4; ++i) { const unsigned o = ror8u(b.e[i]); e0[i] = hi8 ? o : b.e[i]; e1[i] = hi8 ? b.e[i] : o; }
  u32x4 A[3];
  A[0] = u32x4{__builtin_amdgcn_perm(e0[1], e0[0], SEL_LO), __builtin_amdgcn_perm(e0[3], e0[2], SEL_LO),
               __builtin_amdgcn_perm(e1[1], e1[0], SEL_LO), __builtin_amdgcn_perm(e1[3], e1[2], SEL_LO)};
  A[1] = u32x4{__builtin_amdgcn_perm(e0[1], e0[0], selB), __builtin_amdgcn_perm(e0[3], e0[2], selB),
               __builtin_amdgcn_perm(e1[1], e1[0], selB), __builtin_amdgcn_perm(e1[3], e1[2], selB)};
  A[2] = u32x4{__builtin_amdgcn_perm(e0[1], e0[0], SEL_HI), __builtin_amdgcn_perm(e0[3], e0[2], SEL_HI),
               __builtin_amdgcn_perm(e1[1], e1[0], SEL_HI), __builtin_amdgcn_perm(e1[3], e1[2], SEL_HI)};
#pragma unroll
  for (int L = 0; L < 3; ++L)
#pragma unroll
    for (int wd = 0; wd < 4; ++wd) {
#define D_(i) b.d[3 * (i) + L][wd]
      const u32x4 blo = {__builtin_amdgcn_perm(D_(1), D_(0), SEL_LO), __builtin_amdgcn_perm(D_(3), D_(2), SEL_LO),
                         __builtin_amdgcn_perm(D_(5), D_(4), SEL_LO), __builtin_amdgcn_perm(D_(7), D_(6), SEL_LO)};
      const u32x4 bhi = {__builtin_amdgcn_perm(D_(1), D_(0), SEL_HI), __builtin_amdgcn_perm(D_(3), D_(2), SEL_HI),
                         __builtin_amdgcn_perm(D_(5), D_(4), SEL_HI), __builtin_amdgcn_perm(D_(7), D_(6), SEL_HI)};
#undef D_
      acc[L][2 * wd] = mfma32(A[L], blo, acc[L][2 * wd]);
      acc[L][2 * wd + 1] = mfma32(A[L], bhi, acc[L][2 * wd + 1]);
    }
  __builtin_amdgcn_sched_barrier(0);
}

__device__ __forceinline__ void zero_acc(f32x4 (&acc)[3][8]) {
#pragma unroll
  for (int L = 0; L < 3; ++L)
#pragma unroll
    for (int j = 0; j < 8; ++j) acc[L][j] = f32x4{0.f, 0.f, 0.f, 0.f};
}

__device__ __forceinline__ void combine(const f32x4 (&acc)[3][8], f32x4 (&R)[8], bool lo32, bool useB) {
#pragma unroll
  for (int j = 0; j < 8; ++j)
#pragma unroll
    for (int t = 0; t < 4; ++t) {
      const float s1 = useB ? acc[1][j][t] : acc[0][j][t];
      const float s2 = lo32 ? acc[2][j][t] : acc[1][j][t];
      R[j][t] = s1 + ror8(s2);
    }
}

__device__ __forceinline__ void chain_add(f32x4 (&R)[8], LAS f32x4* buf, volatile LAS int* flag, int w, int lane) {
  if (w > 0) {
    while (*flag != w) __builtin_amdgcn_s_sleep(1);
    __builtin_amdgcn_fence(__ATOMIC_ACQUIRE, "workgroup");
#pragma unroll
    for (int j = 0; j < 8; ++j) R[j] += buf[j * 64 + lane];
  }
  if (w < 7) {
#pragma unroll
    for (int j = 0; j < 8; ++j) buf[j * 64 + lane] = R[j];
    __builtin_amdgcn_fence(__ATOMIC_RELEASE, "workgroup");
    *flag = w + 1;
  }
}

__global__ void __launch_bounds__(512, 2)
linformer_kernel(const unsigned char* __restrict__ q, const unsigned char* __restrict__ k, const unsigned char* __restrict__ v,
                 const unsigned char* __restrict__ e, const unsigned char* __restrict__ f, unsigned char* __restrict__ out
                 ) {
  __shared__ __attribute__((aligned(16))) unsigned char lds[8192 + 8192 + 3072 + 6144 + 16];
  LAS unsigned char* L8 = (LAS unsigned char*)lds;
  volatile LAS int* flags = (volatile LAS int*)(L8 + 8192 + 8192 + 3072 + 6144);
  const unsigned t0 = now32();
  const int bh = blockIdx.x;
  const int tid = threadIdx.x, lane = tid & 63, w = __builtin_amdgcn_readfirstlane(tid >> 6);
  const int g = lane >> 4, c = lane & 15;
  if (tid < 3) flags[tid] = tid == 2 ? TOK0 : 0;
  __syncthreads();
  const unsigned tbase = t0 - (DITHER ? ((((bh * 37u) & 63u) * (PACE_G * PACE_TK / 64u)) >> 16) : 0u);
  const bool lo32 = lane < 32, hi8 = c >= 8;
  const bool useB = lo32 && !hi8;
  const int dbase = useB ? (128 + 8 * c) : (8 * c);
  const unsigned loff = 6144u * g + 16u * c;
  const unsigned eoff = 8192u * (c & 7) + 32u * g + (hi8 ? 16u : 0u);
  const unsigned char* kb = k + (size_t)bh * (SKV * ROWB);
  const unsigned char* vb = v + (size_t)bh * (SKV * ROWB);
  LAS u32x4* qs = (LAS u32x4*)(L8 + 8192 + 8192 + 3072);
  if (w == 7) {
#pragma unroll
    for (int s = 0; s < 6; ++s) qs[s * 64 + lane] = *(const GAS u32x4*)(q + (size_t)bh * (SQ * ROWB) + c * ROWB + 64 * s + 16 * g);
  }

  f32x4 acc[3][8], R[8];
  Blk b;
  zero_acc(acc);
#pragma unroll 1
  for (int it = 0; it < 8; ++it) {
    issue(b, kb, e, it * 8 + w, it * 8 + w, loff, eoff, tbase, flags + 2);
    compute(b, acc, hi8);
  }
  combine(acc, R, lo32, useB);
  issue(b, vb, f, w, 64 + w, loff, eoff, tbase, flags + 2);
  chain_add(R, (LAS f32x4*)L8, flags, w, lane);
  u32x2 pb = {0u, 0u};
  if (w == 7) {
    LAS unsigned char* kps = L8 + 16384;
    if (lo32 || !hi8) {
#pragma unroll
      for (int t = 0; t < 4; ++t)
        *(LAS u32x4*)(kps + (4 * (g & 1) + t) * ROWB + dbase * 2) =
            u32x4{pack_bf16(R[0][t], R[1][t]), pack_bf16(R[2][t], R[3][t]), pack_bf16(R[4][t], R[5][t]), pack_bf16(R[6][t], R[7][t])};
    }
    __builtin_amdgcn_fence(__ATOMIC_ACQ_REL, "workgroup");
    f32x4 st = {0.f, 0.f, 0.f, 0.f};
#pragma unroll
    for (int s = 0; s < 6; ++s) st = mfma32(*(const LAS u32x4*)(kps + (c & 7) * ROWB + 64 * s + 16 * g), qs[s * 64 + lane], st);
    float sc[4];
#pragma unroll
    for (int t = 0; t < 4; ++t) sc[t] = round_bf16(round_bf16(st[t]) * 0.07216878364870322f);
    float m = fmaxf(fmaxf(sc[0], sc[1]), fmaxf(sc[2], sc[3]));
    m = fmaxf(m, __shfl_xor(m, 16));
    float ex[4], sum = 0.f;
#pragma unroll
    for (int t = 0; t < 4; ++t) { ex[t] = __expf(sc[t] - m); sum += ex[t]; }
    sum += __shfl_xor(sum, 16);
    const float inv = 1.0f / sum;
    pb = u32x2{pack_bf16(ex[0] * inv, ex[1] * inv), pack_bf16(ex[2] * inv, ex[3] * inv)};
  }
  __builtin_amdgcn_sched_barrier(0);
  zero_acc(acc);
  compute(b, acc, hi8);
#pragma unroll 1
  for (int it = 1; it < 8; ++it) {
    issue(b, vb, f, it * 8 + w, 64 + it * 8 + w, loff, eoff, tbase, flags + 2);
    compute(b, acc, hi8);
  }
  combine(acc, R, lo32, useB);
  chain_add(R, (LAS f32x4*)(L8 + 8192), flags + 1, w, lane);
  if (w == 7) {
    const u32x2 z = {0u, 0u};
    const u32x2 b1 = lo32 ? pb : z, b2 = lo32 ? z : pb;
    const bool valid = lo32 || !hi8;
    f32x4 o1[8], o2[8];
#pragma unroll
    for (int j = 0; j < 8; ++j) {
      u32x2 a = {pack_bf16(R[j][0], R[j][1]), pack_bf16(R[j][2], R[j][3])};
      if (!valid) a = z;
      const u32x2 a1 = lo32 ? a : z, a2 = lo32 ? z : a;
      o1[j] = mfma16(a1, b1, f32x4{0.f, 0.f, 0.f, 0.f});
      o2[j] = mfma16(a2, b2, f32x4{0.f, 0.f, 0.f, 0.f});
    }
    unsigned char* orow = out + (size_t)bh * (SQ * ROWB) + c * ROWB;
    const int d1 = (g < 2) ? (128 + 32 * g) : (32 * g);
#pragma unroll
    for (int t = 0; t < 4; ++t)
      *(GAS u32x4*)(orow + (d1 + 8 * t) * 2) =
          u32x4{pack_bf16(o1[0][t], o1[1][t]), pack_bf16(o1[2][t], o1[3][t]), pack_bf16(o1[4][t], o1[5][t]), pack_bf16(o1[6][t], o1[7][t])};
    if (lo32) {
#pragma unroll
      for (int t = 0; t < 4; ++t)
        *(GAS u32x4*)(orow + (32 * g + 8 * t) * 2) =
            u32x4{pack_bf16(o2[0][t], o2[1][t]), pack_bf16(o2[2][t], o2[3][t]), pack_bf16(o2[4][t], o2[5][t]), pack_bf16(o2[6][t], o2[7][t])};
    }
  }
}

extern "C" void kernel_launch(void* const* d_in, const int* in_sizes, int n_in, void* d_out, int out_size, void* d_ws, size_t ws_size,
                              hipStream_t stream) {
  (void)in_sizes; (void)n_in; (void)out_size; (void)d_ws; (void)ws_size;
  linformer_kernel<<<NBH, 512, 0, stream>>>((const unsigned char*)d_in[0], (const unsigned char*)d_in[1], (const unsigned char*)d_in[2],
                                            (const unsigned char*)d_in[3], (const unsigned char*)d_in[4], (unsigned char*)d_out
  );
}
```

```cpp
#include <hip/hip_runtime.h>
#include <hip/hip_bf16.h>
#include <stdint.h>

typedef unsigned u32x4 __attribute__((ext_vector_type(4)));
typedef unsigned u32x2 __attribute__((ext_vector_type(2)));
typedef float f32x4 __attribute__((ext_vector_type(4)));
typedef short s16x8 __attribute__((ext_vector_type(8)));
typedef short s16x4 __attribute__((ext_vector_type(4)));
#define GAS __attribute__((address_space(1)))
#define LAS __attribute__((address_space(3)))

constexpr int NBH = 256, SQ = 16, SKV = 4096, DH = 192;
constexpr int ROWB = DH * 2;
constexpr int BLKROWS = 64;
constexpr int BLKB = BLKROWS * ROWB;

#define STAMP 0
#define PACE_T 1105
#define PACE_N0 32
#define PACE_G 8
#define DITHER 1
#define TOK0 2
#define ROT 1
#define NOE 0
constexpr unsigned PACE_TK = (unsigned)((unsigned long long)PACE_T * 10ull * 65536ull / 3072ull);

__device__ __forceinline__ f32x4 mfma32(u32x4 a, u32x4 b, f32x4 c) {
  return __builtin_amdgcn_mfma_f32_16x16x32_bf16(__builtin_bit_cast(s16x8, a), __builtin_bit_cast(s16x8, b), c, 0, 0, 0);
}
__device__ __forceinline__ f32x4 mfma16(u32x2 a, u32x2 b, f32x4 c) {
  return __builtin_amdgcn_mfma_f32_16x16x16bf16_1k(__builtin_bit_cast(s16x4, a), __builtin_bit_cast(s16x4, b), c, 0, 0, 0);
}
__device__ __forceinline__ unsigned ror8u(unsigned x) { return (unsigned)__builtin_amdgcn_mov_dpp((int)x, 0x128, 0xf, 0xf, true); }
__device__ __forceinline__ float ror8(float x) { return __builtin_bit_cast(float, ror8u(__builtin_bit_cast(unsigned, x))); }
__device__ __forceinline__ unsigned pack_bf16(float lo, float hi) {
  typedef __bf16 bf2 __attribute__((ext_vector_type(2)));
  bf2 r; r[0] = (__bf16)lo; r[1] = (__bf16)hi;
  return __builtin_bit_cast(unsigned, r);
}
__device__ __forceinline__ float round_bf16(float x) { return (float)(__bf16)x; }
__device__ __forceinline__ unsigned now32() { return (unsigned)__builtin_amdgcn_s_memrealtime(); }

constexpr unsigned SEL_LO = 0x05040100u, SEL_HI = 0x07060302u;

__device__ __forceinline__ void pace(unsigned tbase, int kib) {
  if (PACE_T > 0) {
    const unsigned dl = tbase + (((unsigned)kib * PACE_TK) >> 16);
    while ((int)(dl - now32()) > 0) __builtin_amdgcn_s_sleep(1);
  }
}

struct Blk { u32x4 d[24]; u32x4 e; };

template <bool TOKEN>
__device__ __forceinline__ void issue(Blk& b, __amdgpu_buffer_rsrc_t rx, __amdgpu_buffer_rsrc_t rp, int nb, int m, unsigned loff,
                                      unsigned eoff, unsigned tbase, volatile LAS int* tok) {
  if (TOKEN) { while (*tok != m) __builtin_amdgcn_s_sleep(2); }
#pragma unroll
  for (int s = 0; s < 24 / PACE_G; ++s) {
    pace(tbase, m * 24 + s * PACE_G);
    __builtin_amdgcn_sched_barrier(0);
#pragma unroll
    for (int i = s * PACE_G; i < (s + 1) * PACE_G; ++i) b.d[i] = __builtin_amdgcn_raw_buffer_load_b128(rx, loff, nb * BLKB + 256 * i, 2);
    if (s == 0) b.e = __builtin_amdgcn_raw_buffer_load_b128(rp, eoff, (NOE ? 0 : nb) * (BLKROWS * 2), 0);
    __builtin_amdgcn_sched_barrier(0);
  }
  if (TOKEN) *tok = m + 1;
  __builtin_amdgcn_sched_barrier(0);
}

__device__ __forceinline__ void compute(const Blk& b, f32x4 (&acc)[3][8], bool hi8) {
  const unsigned selB = hi8 ? SEL_HI : SEL_LO;
  u32x4 e0, e1;
#pragma unroll
  for (int i = 0; i < 4; ++i) { const unsigned o = ror8u(b.e[i]); e0[i] = hi8 ? o : b.e[i]; e1[i] = hi8 ? b.e[i] : o; }
  u32x4 A[3];
  A[0] = u32x4{__builtin_amdgcn_perm(e0[1], e0[0], SEL_LO), __builtin_amdgcn_perm(e0[3], e0[2], SEL_LO),
               __builtin_amdgcn_perm(e1[1], e1[0], SEL_LO), __builtin_amdgcn_perm(e1[3], e1[2], SEL_LO)};
  A[1] = u32x4{__builtin_amdgcn_perm(e0[1], e0[0], selB), __builtin_amdgcn_perm(e0[3], e0[2], selB),
               __builtin_amdgcn_perm(e1[1], e1[0], selB), __builtin_amdgcn_perm(e1[3], e1[2], selB)};
  A[2] = u32x4{__builtin_amdgcn_perm(e0[1], e0[0], SEL_HI), __builtin_amdgcn_perm(e0[3], e0[2], SEL_HI),
               __builtin_amdgcn_perm(e1[1], e1[0], SEL_HI), __builtin_amdgcn_perm(e1[3], e1[2], SEL_HI)};
#pragma unroll
  for (int L = 0; L < 3; ++L)
#pragma unroll
    for (int wd = 0; wd < 4; ++wd) {
#define D_(i) b.d[3 * (i) + L][wd]
      const u32x4 blo = {__builtin_amdgcn_perm(D_(1), D_(0), SEL_LO), __builtin_amdgcn_perm(D_(3), D_(2), SEL_LO),
                         __builtin_amdgcn_perm(D_(5), D_(4), SEL_LO), __builtin_amdgcn_perm(D_(7), D_(6), SEL_LO)};
      const u32x4 bhi = {__builtin_amdgcn_perm(D_(1), D_(0), SEL_HI), __builtin_amdgcn_perm(D_(3), D_(2), SEL_HI),
                         __builtin_amdgcn_perm(D_(5), D_(4), SEL_HI), __builtin_amdgcn_perm(D_(7), D_(6), SEL_HI)};
#undef D_
      acc[L][2 * wd] = mfma32(A[L], blo, acc[L][2 * wd]);
      acc[L][2 * wd + 1] = mfma32(A[L], bhi, acc[L][2 * wd + 1]);
    }
  __builtin_amdgcn_sched_barrier(0);
}

__device__ __forceinline__ void zero_acc(f32x4 (&acc)[3][8]) {
#pragma unroll
  for (int L = 0; L < 3; ++L)
#pragma unroll
    for (int j = 0; j < 8; ++j) acc[L][j] = f32x4{0.f, 0.f, 0.f, 0.f};
}

__device__ __forceinline__ void combine(const f32x4 (&acc)[3][8], f32x4 (&R)[8], bool lo32, bool useB) {
#pragma unroll
  for (int j = 0; j < 8; ++j)
#pragma unroll
    for (int t = 0; t < 4; ++t) {
      const float s1 = useB ? acc[1][j][t] : acc[0][j][t];
      const float s2 = lo32 ? acc[2][j][t] : acc[1][j][t];
      R[j][t] = s1 + ror8(s2);
    }
}

__device__ __forceinline__ void chain_add(f32x4 (&R)[8], LAS f32x4* buf, volatile LAS int* flag, int w, int lane) {
  if (w > 0) {
    while (*flag != w) __builtin_amdgcn_s_sleep(1);
    __builtin_amdgcn_fence(__ATOMIC_ACQUIRE, "workgroup");
#pragma unroll
    for (int j = 0; j < 8; ++j) R[j] += buf[j * 64 + lane];
  }
  if (w < 7) {
#pragma unroll
    for (int j = 0; j < 8; ++j) buf[j * 64 + lane] = R[j];
    __builtin_amdgcn_fence(__ATOMIC_RELEASE, "workgroup");
    *flag = w + 1;
  }
}

__global__ void __launch_bounds__(512, 2)
linformer_kernel(const unsigned char* __restrict__ q, const unsigned char* __restrict__ k, const unsigned char* __restrict__ v,
                 const unsigned char* __restrict__ e, const unsigned char* __restrict__ f, unsigned char* __restrict__ out
                 ) {
  __shared__ __attribute__((aligned(16))) unsigned char lds[8192 + 8192 + 3072 + 6144 + 16];
  LAS unsigned char* L8 = (LAS unsigned char*)lds;
  volatile LAS int* flags = (volatile LAS int*)(L8 + 8192 + 8192 + 3072 + 6144);
  const unsigned t0 = now32();
  const int bh = blockIdx.x;
  const int tid = threadIdx.x, lane = tid & 63, w = __builtin_amdgcn_readfirstlane(tid >> 6);
  const int g = lane >> 4, c = lane & 15;
  if (tid < 3) flags[tid] = tid == 2 ? TOK0 : 0;
  const unsigned tbase = t0 - ((PACE_N0 * PACE_TK) >> 16) - (DITHER ? ((((bh * 37u) & 63u) * (PACE_G * PACE_TK / 64u)) >> 16) : 0u);
  const bool lo32 = lane < 32, hi8 = c >= 8;
  const bool useB = lo32 && !hi8;
  const int dbase = useB ? (128 + 8 * c) : (8 * c);
  const unsigned loff = 6144u * g + 16u * c;
  const unsigned eoff = 8192u * (c & 7) + 32u * g + (hi8 ? 16u : 0u);
  const __amdgpu_buffer_rsrc_t rk = __builtin_amdgcn_make_buffer_rsrc((void*)(k + (size_t)bh * (SKV * ROWB)), 0, SKV * ROWB, 0x00020000);
  const __amdgpu_buffer_rsrc_t rv = __builtin_amdgcn_make_buffer_rsrc((void*)(v + (size_t)bh * (SKV * ROWB)), 0, SKV * ROWB, 0x00020000);
  const __amdgpu_buffer_rsrc_t re = __builtin_amdgcn_make_buffer_rsrc((void*)e, 0, 8 * SKV * 2, 0x00020000);
  const __amdgpu_buffer_rsrc_t rf = __builtin_amdgcn_make_buffer_rsrc((void*)f, 0, 8 * SKV * 2, 0x00020000);
  const int rot = w + ROT * bh;
  LAS u32x4* qs = (LAS u32x4*)(L8 + 8192 + 8192 + 3072);

  f32x4 acc[3][8], R[8];
  Blk b;
  if (w < TOK0) issue<false>(b, rk, re, rot & 63, w, loff, eoff, tbase, flags + 2);
  __syncthreads();
  if (w == 7) {
#pragma unroll
    for (int s = 0; s < 6; ++s) qs[s * 64 + lane] = *(const GAS u32x4*)(q + (size_t)bh * (SQ * ROWB) + c * ROWB + 64 * s + 16 * g);
  }
  if (w >= TOK0) issue<true>(b, rk, re, rot & 63, w, loff, eoff, tbase, flags + 2);
  zero_acc(acc);
  compute(b, acc, hi8);
#pragma unroll 1
  for (int it = 1; it < 8; ++it) {
    issue<true>(b, rk, re, (it * 8 + rot) & 63, it * 8 + w, loff, eoff, tbase, flags + 2);
    compute(b, acc, hi8);
  }
  combine(acc, R, lo32, useB);
  issue<true>(b, rv, rf, rot & 63, 64 + w, loff, eoff, tbase, flags + 2);
  chain_add(R, (LAS f32x4*)L8, flags, w, lane);
  u32x2 pb = {0u, 0u};
  if (w == 7) {
    LAS unsigned char* kps = L8 + 16384;
    if (lo32 || !hi8) {
#pragma unroll
      for (int t = 0; t < 4; ++t)
        *(LAS u32x4*)(kps + (4 * (g & 1) + t) * ROWB + dbase * 2) =
            u32x4{pack_bf16(R[0][t], R[1][t]), pack_bf16(R[2][t], R[3][t]), pack_bf16(R[4][t], R[5][t]), pack_bf16(R[6][t], R[7][t])};
    }
    __builtin_amdgcn_fence(__ATOMIC_ACQ_REL, "workgroup");
    f32x4 st = {0.f, 0.f, 0.f, 0.f};
#pragma unroll
    for (int s = 0; s < 6; ++s) st = mfma32(*(const LAS u32x4*)(kps + (c & 7) * ROWB + 64 * s + 16 * g), qs[s * 64 + lane], st);
    float sc[4];
#pragma unroll
    for (int t = 0; t < 4; ++t) sc[t] = round_bf16(round_bf16(st[t]) * 0.07216878364870322f);
    float m = fmaxf(fmaxf(sc[0], sc[1]), fmaxf(sc[2], sc[3]));
    m = fmaxf(m, __shfl_xor(m, 16));
    float ex[4], sum = 0.f;
#pragma unroll
    for (int t = 0; t < 4; ++t) { ex[t] = __expf(sc[t] - m); sum += ex[t]; }
    sum += __shfl_xor(sum, 16);
    const float inv = 1.0f / sum;
    pb = u32x2{pack_bf16(ex[0] * inv, ex[1] * inv), pack_bf16(ex[2] * inv, ex[3] * inv)};
  }
  __builtin_amdgcn_sched_barrier(0);
  zero_acc(acc);
  compute(b, acc, hi8);
#pragma unroll 1
  for (int it = 1; it < 8; ++it) {
    issue<true>(b, rv, rf, (it * 8 + rot) & 63, 64 + it * 8 + w, loff, eoff, tbase, flags + 2);
    compute(b, acc, hi8);
  }
  combine(acc, R, lo32, useB);
  chain_add(R, (LAS f32x4*)(L8 + 8192), flags + 1, w, lane);
  if (w == 7) {
    const u32x2 z = {0u, 0u};
    const u32x2 b1 = lo32 ? pb : z, b2 = lo32 ? z : pb;
    const bool valid = lo32 || !hi8;
    f32x4 o1[8], o2[8];
#pragma unroll
    for (int j = 0; j < 8; ++j) {
      u32x2 a = {pack_bf16(R[j][0], R[j][1]), pack_bf16(R[j][2], R[j][3])};
      if (!valid) a = z;
      const u32x2 a1 = lo32 ? a : z, a2 = lo32 ? z : a;
      o1[j] = mfma16(a1, b1, f32x4{0.f, 0.f, 0.f, 0.f});
      o2[j] = mfma16(a2, b2, f32x4{0.f, 0.f, 0.f, 0.f});
    }
    unsigned char* orow = out + (size_t)bh * (SQ * ROWB) + c * ROWB;
    const int d1 = (g < 2) ? (128 + 32 * g) : (32 * g);
#pragma unroll
    for (int t = 0; t < 4; ++t)
      *(GAS u32x4*)(orow + (d1 + 8 * t) * 2) =
          u32x4{pack_bf16(o1[0][t], o1[1][t]), pack_bf16(o1[2][t], o1[3][t]), pack_bf16(o1[4][t], o1[5][t]), pack_bf16(o1[6][t], o1[7][t])};
    if (lo32) {
#pragma unroll
      for (int t = 0; t < 4; ++t)
        *(GAS u32x4*)(orow + (32 * g + 8 * t) * 2) =
            u32x4{pack_bf16(o2[0][t], o2[1][t]), pack_bf16(o2[2][t], o2[3][t]), pack_bf16(o2[4][t], o2[5][t]), pack_bf16(o2[6][t], o2[7][t])};
    }
  }
}

extern "C" void kernel_launch(void* const* d_in, const int* in_sizes, int n_in, void* d_out, int out_size, void* d_ws, size_t ws_size,
                              hipStream_t stream) {
  (void)in_sizes; (void)n_in; (void)out_size; (void)d_ws; (void)ws_size;
  linformer_kernel<<<NBH, 512, 0, stream>>>((const unsigned char*)d_in[0], (const unsigned char*)d_in[1], (const unsigned char*)d_in[2],
                                            (const unsigned char*)d_in[3], (const unsigned char*)d_in[4], (unsigned char*)d_out
  );
}
```

```cpp
#include <hip/hip_runtime.h>
#include <hip/hip_bf16.h>
#include <stdint.h>

typedef unsigned u32x4 __attribute__((ext_vector_type(4)));
typedef unsigned u32x2 __attribute__((ext_vector_type(2)));
typedef float f32x4 __attribute__((ext_vector_type(4)));
typedef short s16x8 __attribute__((ext_vector_type(8)));
typedef short s16x4 __attribute__((ext_vector_type(4)));
typedef unsigned long long u64;
#define GAS __attribute__((address_space(1)))
#define LAS __attribute__((address_space(3)))

constexpr int NWG = 256, SQ = 16, SKV = 4096, DH = 192;
constexpr int ROWB = DH * 2;
constexpr int BHB = SKV * ROWB;

#define STAMP 0
#define PACE_T 1085
#define PACE_N0 32
#define TOK0 2
#define ROT 17
constexpr unsigned PACE_TK = (unsigned)((u64)PACE_T * 10ull * 65536ull / 3072ull);

constexpr int WS_FLAG = 0, WS_ACK = NWG * 256, WS_PAY = WS_ACK + NWG * 256, WS_PAY_STRIDE = 2048;
constexpr u64 MAGIC = 0x9E3779B97F4A7C15ull;
constexpr unsigned SPIN_TICKS = 5000000u;

constexpr int L_CHAIN = 0;
constexpr int L_KP = 2048;
constexpr int L_ST = 3072;
constexpr int L_PB = 6144;
constexpr int L_FLAGS = 7680;
constexpr int L_BYTES = 84 * 1024;

__device__ __forceinline__ f32x4 mfma32(u32x4 a, u32x4 b, f32x4 c) {
  return __builtin_amdgcn_mfma_f32_16x16x32_bf16(__builtin_bit_cast(s16x8, a), __builtin_bit_cast(s16x8, b), c, 0, 0, 0);
}
__device__ __forceinline__ f32x4 mfma16(u32x2 a, u32x2 b, f32x4 c) {
  return __builtin_amdgcn_mfma_f32_16x16x16bf16_1k(__builtin_bit_cast(s16x4, a), __builtin_bit_cast(s16x4, b), c, 0, 0, 0);
}
__device__ __forceinline__ float ror8(float x) {
  return __builtin_bit_cast(float, __builtin_amdgcn_mov_dpp(__builtin_bit_cast(int, x), 0x128, 0xf, 0xf, true));
}
__device__ __forceinline__ float fold_halves(float a, float b) {
  const auto sw = __builtin_amdgcn_permlane32_swap(__float_as_uint(a), __float_as_uint(b), false, false);
  const unsigned lo = sw[0], hi = sw[1];
  return __uint_as_float(lo) + ror8(__uint_as_float(hi));
}
__device__ __forceinline__ float merge_hi8(float a, float b) {
  return __int_as_float(__builtin_amdgcn_update_dpp(__float_as_int(a), __float_as_int(b), 0x128, 0xf, 0xc, false));
}
__device__ __forceinline__ unsigned pack_bf16(float lo, float hi) {
  typedef __bf16 bf2 __attribute__((ext_vector_type(2)));
  bf2 r; r[0] = (__bf16)lo; r[1] = (__bf16)hi;
  return __builtin_bit_cast(unsigned, r);
}
__device__ __forceinline__ float round_bf16(float x) { return (float)(__bf16)x; }
__device__ __forceinline__ unsigned now32() { return (unsigned)__builtin_amdgcn_s_memrealtime(); }
__device__ __forceinline__ u64 ld_agent(const unsigned char* p) { return __hip_atomic_load((const GAS u64*)p, __ATOMIC_RELAXED, __HIP_MEMORY_SCOPE_AGENT); }
__device__ __forceinline__ void st_agent(unsigned char* p, u64 v) { __hip_atomic_store((GAS u64*)p, v, __ATOMIC_RELAXED, __HIP_MEMORY_SCOPE_AGENT); }

constexpr unsigned SEL_LO = 0x05040100u, SEL_HI = 0x07060302u;

__device__ __forceinline__ void wait_words(const unsigned char* p, u64 want, int lane) {
  const unsigned t0 = now32();
  for (;;) {
    bool ok = true;
    if (lane < 9) ok = ld_agent(p) == want;
    if (__all(ok)) break;
    if (now32() - t0 > SPIN_TICKS) break;
    __builtin_amdgcn_s_sleep(8);
  }
}

__global__ void __launch_bounds__(512, 2)
linformer_kernel(const unsigned char* __restrict__ q, const unsigned char* __restrict__ k, const unsigned char* __restrict__ v,
                 const unsigned char* __restrict__ e, const unsigned char* __restrict__ f, unsigned char* __restrict__ out,
                 unsigned char* ws
                 ) {
  __shared__ __attribute__((aligned(16))) unsigned char lds[L_BYTES];
  LAS unsigned char* L8 = (LAS unsigned char*)lds;
  volatile LAS int* tok = (volatile LAS int*)(L8 + L_FLAGS);
  volatile LAS int* chain = (volatile LAS int*)(L8 + L_FLAGS + 4);
  const unsigned tbase = now32() - ((PACE_N0 * PACE_TK) >> 16);
  const int cu = (blockIdx.x & 7) * 32 + (blockIdx.x >> 3);
  const int tid = threadIdx.x, lane = tid & 63, w = __builtin_amdgcn_readfirstlane(tid >> 6);
  const int g = lane >> 4, c = lane & 15;
  const bool lo32 = lane < 32;
  const unsigned doff = (lane >> 3) * (8u * ROWB) + (lane & 7) * 16u;
  const unsigned eoff = (lane & 7) * (SKV * 2u) + (lane >> 3) * 16u;

  u64 old = 0;
  int pcu = 0, pslot = 0;
  if (w == 7) {
    if (lane < 2) *(volatile LAS int*)(L8 + L_FLAGS + 4 * lane) = lane == 0 ? TOK0 : 0;
    const int lj = lane < 9 ? lane / 3 : 0, lp = lane < 9 ? lane - 3 * lj : 0;
    const int u = 256 * lj + cu, ub = u / 3, up = u - 3 * ub, pu = 3 * ub + lp;
    pcu = pu & 255;
    pslot = 3 * (pu >> 8) + up;
  }

  f32x4 acc[8];
#pragma unroll
  for (int j = 0; j < 8; ++j) acc[j] = f32x4{0.f, 0.f, 0.f, 0.f};

#pragma unroll 1
  for (int ph = 0; ph < 6; ++ph) {
    const int tn = ph >= 3, j3 = ph - 3 * tn;
    const int u = 256 * j3 + cu, bh = u / 3, p = u - 3 * bh;
    const __amdgpu_buffer_rsrc_t rx =
        __builtin_amdgcn_make_buffer_rsrc((void*)((tn ? v : k) + (size_t)bh * BHB + p * 128), 0, BHB - p * 128, 0x00020000);
    const __amdgpu_buffer_rsrc_t rp = __builtin_amdgcn_make_buffer_rsrc((void*)(tn ? f : e), 0, 8 * SKV * 2, 0x00020000);
#pragma unroll 1
    for (int i4 = 0; i4 < 4; ++i4) {
      const int m = (ph * 4 + i4) * 8 + w;
      if (m < 8 && m >= TOK0) {
        __syncthreads();
        if (w == 7) {
          if (lane < 9) { old = ld_agent(ws + WS_FLAG + pcu * 256); st_agent(ws + WS_ACK + cu * 256 + lane * 8, old ^ MAGIC); }
        }
      }
      if (m >= TOK0) { while (*tok != m) __builtin_amdgcn_s_sleep(2); }
      u32x4 d[16], ef[2];
#pragma unroll
      for (int s = 0; s < 2; ++s) {
        const int rb = (2 * m + s + ROT * bh) & 63;
        if (PACE_T > 0) {
          const unsigned dl = tbase + (((unsigned)(16 * m + 8 * s) * PACE_TK) >> 16);
          while ((int)(dl - now32()) > 0) __builtin_amdgcn_s_sleep(1);
        }
        __builtin_amdgcn_sched_barrier(0);
#pragma unroll
        for (int i = 0; i < 8; ++i) d[8 * s + i] = __builtin_amdgcn_raw_buffer_load_b128(rx, doff, rb * (64 * ROWB) + i * ROWB, 2);
        ef[s] = __builtin_amdgcn_raw_buffer_load_b128(rp, eoff, rb * 128, 0);
        __builtin_amdgcn_sched_barrier(0);
      }
      if (m >= TOK0) *tok = m + 1;
      if (m < TOK0) __syncthreads();
      __builtin_amdgcn_sched_barrier(0);
#pragma unroll
      for (int s = 0; s < 2; ++s)
#pragma unroll
        for (int wd = 0; wd < 4; ++wd) {
#define D_(i) d[8 * s + (i)][wd]
          const u32x4 blo = {__builtin_amdgcn_perm(D_(1), D_(0), SEL_LO), __builtin_amdgcn_perm(D_(3), D_(2), SEL_LO),
                             __builtin_amdgcn_perm(D_(5), D_(4), SEL_LO), __builtin_amdgcn_perm(D_(7), D_(6), SEL_LO)};
          const u32x4 bhi = {__builtin_amdgcn_perm(D_(1), D_(0), SEL_HI), __builtin_amdgcn_perm(D_(3), D_(2), SEL_HI),
                             __builtin_amdgcn_perm(D_(5), D_(4), SEL_HI), __builtin_amdgcn_perm(D_(7), D_(6), SEL_HI)};
#undef D_
          acc[2 * wd] = mfma32(ef[s], blo, acc[2 * wd]);
          acc[2 * wd + 1] = mfma32(ef[s], bhi, acc[2 * wd + 1]);
        }
      __builtin_amdgcn_sched_barrier(0);
      if (w == 7 && i4 < 3) {
        if (ph == 2 && i4 == 0) {
          const u64 mine = __builtin_bit_cast(u64, u32x2{(unsigned)__builtin_amdgcn_readlane((int)(unsigned)old, cu % 3),
                                                         (unsigned)__builtin_amdgcn_readlane((int)(unsigned)(old >> 32), cu % 3)});
          wait_words(ws + WS_ACK + pcu * 256 + pslot * 8, mine ^ MAGIC, lane);
        }
        if (ph == 3 && i4 == 0) {
          asm volatile("s_waitcnt vmcnt(0)" ::: "memory");
          if (lane == cu % 3) st_agent(ws + WS_FLAG + cu * 256, old + 1);
        }
        if (ph == 3 && i4 == 1) {
          wait_words(ws + WS_FLAG + pcu * 256, old + 1, lane);
          __builtin_amdgcn_fence(__ATOMIC_ACQUIRE, "wavefront");
        }
        if (ph == 3 && i4 == 2) {
          const __amdgpu_buffer_rsrc_t rw = __builtin_amdgcn_make_buffer_rsrc((void*)(ws + WS_PAY), 0, NWG * WS_PAY_STRIDE, 0x00020000);
          u32x4 pl[9];
#pragma unroll
          for (int l = 0; l < 9; ++l) {
            const int pu = ((256 * (l / 3) + cu) / 3) * 3 + l % 3;
            pl[l] = __builtin_amdgcn_raw_buffer_load_b128(rw, (lane & 31) * 16, (pu & 255) * WS_PAY_STRIDE + (pu >> 8) * 512, 16);
          }
#pragma unroll
          for (int jj = 0; jj < 3; ++jj) {
            const f32x4 st = (__builtin_bit_cast(f32x4, pl[3 * jj]) + __builtin_bit_cast(f32x4, pl[3 * jj + 1])) + __builtin_bit_cast(f32x4, pl[3 * jj + 2]);
            float sc[4];
#pragma unroll
            for (int t = 0; t < 4; ++t) sc[t] = round_bf16(round_bf16(st[t]) * 0.07216878364870322f);
            float mx = fmaxf(fmaxf(sc[0], sc[1]), fmaxf(sc[2], sc[3]));
            mx = fmaxf(mx, __shfl_xor(mx, 16));
            float ex[4], sum = 0.f;
#pragma unroll
            for (int t = 0; t < 4; ++t) { ex[t] = __expf(sc[t] - mx); sum += ex[t]; }
            sum += __shfl_xor(sum, 16);
            const float inv = 1.0f / sum;
            *(LAS u32x2*)(L8 + L_PB + (jj * 64 + lane) * 8) = u32x2{pack_bf16(ex[0] * inv, ex[1] * inv), pack_bf16(ex[2] * inv, ex[3] * inv)};
          }
        }
      }
    }
    f32x4 Y[2];
#pragma unroll
    for (int x = 0; x < 2; ++x)
#pragma unroll
      for (int t = 0; t < 4; ++t) {
        float X[2];
#pragma unroll
        for (int h = 0; h < 2; ++h) {
          X[h] = fold_halves(acc[4 * h + x][t], acc[4 * h + x + 2][t]);
        }
        Y[x][t] = merge_hi8(X[0], X[1]);
      }
#pragma unroll
    for (int j = 0; j < 8; ++j) acc[j] = f32x4{0.f, 0.f, 0.f, 0.f};
    LAS f32x4* cb = (LAS f32x4*)(L8 + L_CHAIN);
    while (*chain != ph * 8 + w) __builtin_amdgcn_s_sleep(1);
    __builtin_amdgcn_fence(__ATOMIC_ACQUIRE, "workgroup");
    if (w > 0) { Y[0] += cb[lane]; Y[1] += cb[64 + lane]; }
    if (w < 7) { cb[lane] = Y[0]; cb[64 + lane] = Y[1]; }
    asm volatile("" : "+v"(Y[0]), "+v"(Y[1]));
    __builtin_amdgcn_fence(__ATOMIC_RELEASE, "workgroup");
    *chain = ph * 8 + w + 1;
    if (w == 7) {
      if (!tn) {
        const unsigned char* qp = q + (size_t)bh * (SQ * ROWB) + c * ROWB + p * 128 + g * 16;
        const u32x4 q0 = __builtin_nontemporal_load((const GAS u32x4*)qp), q1 = __builtin_nontemporal_load((const GAS u32x4*)(qp + 64));
#pragma unroll
        for (int t = 0; t < 4; ++t)
          *(LAS unsigned*)(L8 + L_KP + (4 * (g & 1) + t) * 128 + (c & 7) * 16 + (c >> 3) * 8 + (g >> 1) * 4) = pack_bf16(Y[0][t], Y[1][t]);
        __builtin_amdgcn_fence(__ATOMIC_ACQ_REL, "workgroup");
        f32x4 st = mfma32(*(const LAS u32x4*)(L8 + L_KP + (c & 7) * 128 + g * 16), q0, f32x4{0.f, 0.f, 0.f, 0.f});
        st = mfma32(*(const LAS u32x4*)(L8 + L_KP + (c & 7) * 128 + 64 + g * 16), q1, st);
        *(LAS f32x4*)(L8 + L_ST + (j3 * 64 + lane) * 16) = st;
        if (ph == 2) {
          __builtin_amdgcn_fence(__ATOMIC_ACQ_REL, "workgroup");
          const __amdgpu_buffer_rsrc_t rw = __builtin_amdgcn_make_buffer_rsrc((void*)(ws + WS_PAY + cu * WS_PAY_STRIDE), 0, WS_PAY_STRIDE, 0x00020000);
          const u32x4 a = *(const LAS u32x4*)(L8 + L_ST + ((lane >> 5) * 64 + (lane & 31)) * 16);
          __builtin_amdgcn_raw_buffer_store_b128(a, rw, lane * 16, 0, 16);
          if (lo32) __builtin_amdgcn_raw_buffer_store_b128(__builtin_bit_cast(u32x4, st), rw, lane * 16, 1024, 16);
        }
      } else {
        const u32x2 pb = *(const LAS u32x2*)(L8 + L_PB + (j3 * 64 + lane) * 8);
        const u32x2 z = {0u, 0u};
        const u32x2 b0 = lo32 ? pb : z, b1 = lo32 ? z : pb;
        f32x4 o[4];
#pragma unroll
        for (int x = 0; x < 2; ++x) {
          const u32x2 a = {pack_bf16(Y[x][0], Y[x][1]), pack_bf16(Y[x][2], Y[x][3])};
          o[x] = mfma16(lo32 ? a : z, b0, f32x4{0.f, 0.f, 0.f, 0.f});
          o[2 + x] = mfma16(lo32 ? z : a, b1, f32x4{0.f, 0.f, 0.f, 0.f});
        }
        unsigned char* op = out + (size_t)bh * (SQ * ROWB) + c * ROWB + p * 128 + (g & 1) * 64 + (g >> 1) * 8;
#pragma unroll
        for (int t = 0; t < 4; ++t)
          *(GAS u32x2*)(op + t * 16) = u32x2{pack_bf16(o[0][t], o[1][t]), pack_bf16(o[2][t], o[3][t])};
      }
    }
  }
}

extern "C" void kernel_launch(void* const* d_in, const int* in_sizes, int n_in, void* d_out, int out_size, void* d_ws, size_t ws_size,
                              hipStream_t stream) {
  (void)in_sizes; (void)n_in; (void)out_size; (void)ws_size;
  linformer_kernel<<<NWG, 512, 0, stream>>>((const unsigned char*)d_in[0], (const unsigned char*)d_in[1], (const unsigned char*)d_in[2],
                                            (const unsigned char*)d_in[3], (const unsigned char*)d_in[4], (unsigned char*)d_out,
                                            (unsigned char*)d_ws
  );
}
```

```cpp
#include <hip/hip_runtime.h>
#include <hip/hip_bf16.h>
#include <stdint.h>

typedef unsigned u32x4 __attribute__((ext_vector_type(4)));
typedef unsigned u32x2 __attribute__((ext_vector_type(2)));
typedef float f32x4 __attribute__((ext_vector_type(4)));
typedef short s16x8 __attribute__((ext_vector_type(8)));
typedef short s16x4 __attribute__((ext_vector_type(4)));
typedef unsigned long long u64;
typedef __amdgpu_buffer_rsrc_t rsrc_t;
#define GAS __attribute__((address_space(1)))
#define LAS __attribute__((address_space(3)))

constexpr int NWG = 256, SQ = 16, SKV = 4096, DH = 192;
constexpr int ROWB = DH * 2;
constexpr int BHB = SKV * ROWB;

constexpr int PACE_T = 1036;
constexpr int GAP_A = 45, AT_A = 9;
constexpr int GAP_Q = 35, GAP_P = 30;
constexpr int GAP_T = 6;
constexpr int ROTF = 3016;
constexpr int DMUL = 24;
constexpr int TOK0 = 2;
constexpr int AUX_NT = 2, AUX_DEV = 16, AUX_DEV_NT = 18;
constexpr int AT_TLB = 12, AT_ACK = 14, AT_Q = 18, AT_ACKCHK = 70, AT_PUB = 102, AT_FLAG = 108, AT_POLL = 116, AT_PAY = 122, AT_OUT = 184;
constexpr int C_B0 = 28, C_PER = 16, C_E0 = 2, C_A = 25, C_B = 60, C_POS = 300;
constexpr int C_PUB = 20, C_LD = 3, C_RD = 7, C_CAP = 500, C_STEP = 12;
constexpr unsigned PACE_TB = (unsigned)((u64)PACE_T * 10ull * 65536ull / 192ull);
__device__ __forceinline__ unsigned sched(int blk) {
  return (((unsigned)blk * PACE_TB) >> 16) + (blk > AT_A ? GAP_A : 0) + (blk > AT_Q ? GAP_Q : 0) + (blk > AT_PUB ? GAP_P : 0) + (blk >> 5) * GAP_T;
}
#define THIRD(i) (((i) + 2) % 3)

constexpr int WS_FLAG = 0, WS_ACK = NWG * 128, WS_PAY = WS_ACK + NWG * 128, WS_PAY_STRIDE = 1536, WS_T0 = WS_PAY + NWG * WS_PAY_STRIDE;
constexpr int WS_FAR = 4352, WS_LAT = WS_T0 + 8 * WS_FAR, WS_LAT_STRIDE = 1280, WS_CMD = WS_LAT + 64 * WS_LAT_STRIDE;
constexpr int WS_ANC = WS_CMD + 16 * WS_FAR, WS_BYTES = WS_ANC + 32 * WS_LAT_STRIDE;
constexpr unsigned MAGIC_LO = 0x7F4A7C15u, MAGIC_HI = 0x9E3779B9u;
constexpr unsigned SPIN_TICKS = 5000000u;

constexpr int L_CHAIN = 0;
constexpr int L_KP = 6144;
constexpr int L_PB = 7168;
constexpr int L_FLAGS = 7680;
constexpr int L_BYTES = 84 * 1024;

__device__ __forceinline__ f32x4 mfma32(u32x4 a, u32x4 b, f32x4 c) {
  return __builtin_amdgcn_mfma_f32_16x16x32_bf16(__builtin_bit_cast(s16x8, a), __builtin_bit_cast(s16x8, b), c, 0, 0, 0);
}
__device__ __forceinline__ f32x4 mfma16(u32x2 a, u32x2 b, f32x4 c) {
  return __builtin_amdgcn_mfma_f32_16x16x16bf16_1k(__builtin_bit_cast(s16x4, a), __builtin_bit_cast(s16x4, b), c, 0, 0, 0);
}
__device__ __forceinline__ float ror8(float x) { return __int_as_float(__builtin_amdgcn_mov_dpp(__float_as_int(x), 0x128, 0xf, 0xf, true)); }
__device__ __forceinline__ unsigned take_hi8(unsigned a, unsigned b) { return (unsigned)__builtin_amdgcn_update_dpp((int)a, (int)b, 0x128, 0xf, 0xc, false); }
__device__ __forceinline__ unsigned take_lo8(unsigned a, unsigned b) { return (unsigned)__builtin_amdgcn_update_dpp((int)a, (int)b, 0x128, 0xf, 0x3, false); }
__device__ __forceinline__ float fold_halves(float a, float b) {
  const auto sw = __builtin_amdgcn_permlane32_swap(__float_as_uint(a), __float_as_uint(b), false, false);
  const unsigned lo = sw[0], hi = sw[1];
  return __uint_as_float(lo) + ror8(__uint_as_float(hi));
}
__device__ __forceinline__ unsigned pack_bf16(float lo, float hi) {
  typedef __bf16 bf2 __attribute__((ext_vector_type(2)));
  bf2 r; r[0] = (__bf16)lo; r[1] = (__bf16)hi;
  return __builtin_bit_cast(unsigned, r);
}
__device__ __forceinline__ float round_bf16(float x) { return (float)(__bf16)x; }
__device__ __forceinline__ unsigned now32() { return (unsigned)__builtin_amdgcn_s_memrealtime(); }
__device__ __forceinline__ unsigned uni(int x) { return (unsigned)__builtin_amdgcn_readfirstlane(x); }
__device__ __forceinline__ rsrc_t make_rsrc(const void* p, int bytes) { return __builtin_amdgcn_make_buffer_rsrc((void*)p, 0, bytes, 0x00020000); }

constexpr unsigned SEL_LO = 0x05040100u, SEL_HI = 0x07060302u;

__device__ __forceinline__ void wait_words(rsrc_t rw, int off, u32x2 want, int lane) {
  const unsigned t0 = now32();
  for (;;) {
    asm volatile("" ::: "memory");
    bool ok = true;
    if (lane < 3) { const u32x2 x = __builtin_amdgcn_raw_buffer_load_b64(rw, off, 0, AUX_DEV_NT); ok = x[0] == want[0] && x[1] == want[1]; }
    if (__all(ok)) break;
    if (now32() - t0 > SPIN_TICKS) break;
    __builtin_amdgcn_s_sleep(32);
  }
  asm volatile("" ::: "memory");
}
__device__ __forceinline__ void lds_wait_eq(volatile LAS int* p, int want) {
  while (*p != want) __builtin_amdgcn_s_sleep(8);
  __builtin_amdgcn_fence(__ATOMIC_ACQUIRE, "workgroup");
}
__device__ __forceinline__ void wait_instant(volatile LAS int* tbp, int blk) {
  const unsigned off = sched(blk);
  while ((int)(uni(*tbp) + off - now32()) > 40) __builtin_amdgcn_s_sleep(8);
  const unsigned dl = uni(*tbp) + off;
  while ((int)(dl - now32()) > 0) {}
  asm volatile("" ::: "memory");
}

__device__ __forceinline__ void pv_unit(const f32x4 (&Y)[2], u32x2 pb, bool lo32, u32x4& x, u32x4& y) {
  const u32x2 z = {0u, 0u};
  const u32x2 b0 = lo32 ? pb : z, b1 = lo32 ? z : pb;
  f32x4 o[4];
#pragma unroll
  for (int i = 0; i < 2; ++i) {
    const u32x2 a = {pack_bf16(Y[i][0], Y[i][1]), pack_bf16(Y[i][2], Y[i][3])};
    o[i] = mfma16(lo32 ? a : z, b0, f32x4{0.f, 0.f, 0.f, 0.f});
    o[2 + i] = mfma16(lo32 ? z : a, b1, f32x4{0.f, 0.f, 0.f, 0.f});
  }
  u32x4 w4[2];
#pragma unroll
  for (int kk = 0; kk < 2; ++kk) {
    const auto s0 = __builtin_amdgcn_permlane32_swap(pack_bf16(o[0][2 * kk], o[1][2 * kk]), pack_bf16(o[0][2 * kk + 1], o[1][2 * kk + 1]), false, false);
    const auto s1 = __builtin_amdgcn_permlane32_swap(pack_bf16(o[2][2 * kk], o[3][2 * kk]), pack_bf16(o[2][2 * kk + 1], o[3][2 * kk + 1]), false, false);
    w4[kk] = u32x4{s0[0], s1[0], s0[1], s1[1]};
  }
#pragma unroll
  for (int i = 0; i < 4; ++i) { x[i] = take_hi8(w4[0][i], w4[1][i]); y[i] = take_lo8(w4[1][i], w4[0][i]); }
}
__device__ __forceinline__ void store_unit(unsigned char* out, int bh, int p, int lane, u32x4 x, u32x4 y) {
  const int g = lane >> 4, c = lane & 15;
  const rsrc_t ro = make_rsrc(out + (size_t)bh * (SQ * ROWB), SQ * ROWB);
  const int off = (c & 7) * ROWB + p * 128 + (g & 1) * 64 + (c >> 3) * 32 + (g >> 1) * 16;
  __builtin_amdgcn_raw_buffer_store_b128(x, ro, off, 0, 0);
  __builtin_amdgcn_raw_buffer_store_b128(y, ro, off, 8 * ROWB, 0);
}
__device__ __forceinline__ int unit_bh(int j, int t) { return 85 * j + (j == 2) + t; }
constexpr int SOLO_BH = 170, SOLO_GRP = 342;
__device__ __forceinline__ void load_block(u32x4 (&d)[16], u32x4 (&ef)[2], rsrc_t rx, rsrc_t rp, int lane, int grp0) {
  unsigned doff[2], eoff[2];
#pragma unroll
  for (int s = 0; s < 2; ++s) {
    const unsigned grp = (unsigned)(grp0 + 8 * s + (lane >> 3)) & 511u;
    doff[s] = grp * (8u * ROWB) + (lane & 7) * 16u;
    eoff[s] = (lane & 7) * (SKV * 2u) + grp * 16u;
  }
  __builtin_amdgcn_sched_barrier(0);
#pragma unroll
  for (int i = 0; i < 16; ++i) d[i] = __builtin_amdgcn_raw_buffer_load_b128(rx, doff[i >> 3] + (i & 7) * ROWB, 0, AUX_NT);
  ef[0] = __builtin_amdgcn_raw_buffer_load_b128(rp, eoff[0], 0, 0);
  ef[1] = __builtin_amdgcn_raw_buffer_load_b128(rp, eoff[1], 0, 0);
  __builtin_amdgcn_sched_barrier(0);
}

__global__ void __launch_bounds__(640, 3)
linformer_kernel(const unsigned char* __restrict__ k, const unsigned char* __restrict__ e, const unsigned char* __restrict__ v,
                 const unsigned char* __restrict__ f, const unsigned char* __restrict__ q, unsigned char* __restrict__ out,
                 unsigned char* ws) {
  __shared__ __attribute__((aligned(16))) unsigned char lds[L_BYTES];
  LAS unsigned char* L8 = (LAS unsigned char*)lds;
  volatile LAS int* tok = (volatile LAS int*)(L8 + L_FLAGS);
  volatile LAS int* tbp = (volatile LAS int*)(L8 + L_FLAGS + 4);
  volatile LAS int* pbok = (volatile LAS int*)(L8 + L_FLAGS + 8);
  volatile LAS int* latp = (volatile LAS int*)(L8 + L_FLAGS + 12);
  volatile LAS int* chain = (volatile LAS int*)(L8 + L_FLAGS + 16);
  LAS f32x4* cb = (LAS f32x4*)(L8 + L_CHAIN);
  const int bx = blockIdx.x;
  const int cu = (bx & 7) * 32 + (bx >> 3);
  const int tid = threadIdx.x, lane = tid & 63, w = __builtin_amdgcn_readfirstlane(tid >> 6);
  const bool solo = cu == 255;
  const int tri = cu / 3, pw = solo ? w % 3 : cu - 3 * tri;
  u32x4 d[16], ef[2];
  if (w < TOK0) {
    const int bh = solo ? SOLO_BH : unit_bh(2, tri), g0 = solo ? SOLO_GRP : 16 * w + ((ROTF * tri) >> 4);
    const rsrc_t rx = make_rsrc(k + (size_t)bh * BHB + pw * 128, BHB - pw * 128), rp = make_rsrc(e, 8 * SKV * 2);
    load_block(d, ef, rx, rp, lane, g0);
  }
  const bool lo32 = lane < 32;
  unsigned t0 = 0;
  if (w == 9) {
    t0 = now32();
    if (lane < 7) *(volatile LAS int*)(L8 + L_FLAGS + 4 * lane) = lane == 0 ? TOK0 : (lane == 1 ? (int)t0 : 0);
  }
  __syncthreads();

  if (__builtin_expect(w < 8, 1)) {
    const unsigned dith = ((((unsigned)(tri * DMUL) & 255u) * (PACE_TB / 256u)) >> 16) - (PACE_TB >> 17);
    f32x4 acc[8], pre[2] = {f32x4{0.f, 0.f, 0.f, 0.f}, f32x4{0.f, 0.f, 0.f, 0.f}};
    u32x2 pbl = {0u, 0u};
#pragma unroll
    for (int j = 0; j < 8; ++j) acc[j] = f32x4{0.f, 0.f, 0.f, 0.f};
    if (solo && w >= 6) return;
    const int nseg = solo ? 2 : 6, nblk = solo ? 16 : 4, nwv = solo ? 6 : 8;
    volatile LAS int* ch = chain + (solo ? pw : 0);
    LAS f32x4* cw = cb + (solo ? pw : 0) * 128 + lane;
#pragma unroll 1
    for (int ph = 0; ph < nseg; ++ph) {
      const int tn = 2 * ph >= nseg;
      const int bh = solo ? SOLO_BH : unit_bh(THIRD(ph - 3 * tn), tri);
      const rsrc_t rx = make_rsrc((tn ? v : k) + (size_t)bh * BHB + pw * 128, BHB - pw * 128), rp = make_rsrc(tn ? f : e, 8 * SKV * 2);
      const int turn = solo ? 3 * ph + (w >= 3) : 9 * ph + w;
      const bool fin = ph == nseg - 1 && w >= (solo ? 3 : 7);
#pragma unroll 1
      for (int i4 = 0; i4 < nblk; ++i4) {
        const int m = (ph * nblk + i4) * nwv + w;
        if (m >= TOK0) {
          const int g0 = solo ? SOLO_GRP + 16 * (m / 3) : 16 * m + ((ROTF * tri) >> 4);
          while (*tok != m) __builtin_amdgcn_s_sleep(2);
          const unsigned dl = uni(*tbp) - dith + sched(solo ? 3 * (m / 3) : m);
          int rem = (int)(dl - now32());
          *latp = -rem;
          while (rem > 0) { if (rem > 8) __builtin_amdgcn_s_sleep(1); rem = (int)(dl - now32()); }
          load_block(d, ef, rx, rp, lane, g0);
          *tok = m + 1;
        }
        if (fin && i4 == nblk - 1) {
          lds_wait_eq(pbok, 1);
          pbl = *(const LAS u32x2*)(L8 + L_PB + lane * 8);
          while (*ch != turn) __builtin_amdgcn_s_sleep(1);
          __builtin_amdgcn_fence(__ATOMIC_ACQUIRE, "workgroup");
          pre[0] = cw[0]; pre[1] = cw[64];
        }
        __builtin_amdgcn_sched_barrier(0);
#pragma unroll
        for (int s = 0; s < 2; ++s)
#pragma unroll
          for (int wd = 0; wd < 4; ++wd) {
#define D_(i) d[8 * s + (i)][wd]
            const u32x4 blo = {__builtin_amdgcn_perm(D_(1), D_(0), SEL_LO), __builtin_amdgcn_perm(D_(3), D_(2), SEL_LO),
                               __builtin_amdgcn_perm(D_(5), D_(4), SEL_LO), __builtin_amdgcn_perm(D_(7), D_(6), SEL_LO)};
            const u32x4 bhi = {__builtin_amdgcn_perm(D_(1), D_(0), SEL_HI), __builtin_amdgcn_perm(D_(3), D_(2), SEL_HI),
                               __builtin_amdgcn_perm(D_(5), D_(4), SEL_HI), __builtin_amdgcn_perm(D_(7), D_(6), SEL_HI)};
#undef D_
            acc[2 * wd] = mfma32(ef[s], blo, acc[2 * wd]);
            acc[2 * wd + 1] = mfma32(ef[s], bhi, acc[2 * wd + 1]);
          }
        __builtin_amdgcn_sched_barrier(0);
      }
      f32x4 Y[2];
#pragma unroll
      for (int x = 0; x < 2; ++x)
#pragma unroll
        for (int t = 0; t < 4; ++t)
          Y[x][t] = __uint_as_float(take_hi8(__float_as_uint(fold_halves(acc[x][t], acc[x + 2][t])), __float_as_uint(fold_halves(acc[4 + x][t], acc[6 + x][t]))));
#pragma unroll
      for (int j = 0; j < 8; ++j) acc[j] = f32x4{0.f, 0.f, 0.f, 0.f};
      if (fin) {
        Y[0] += pre[0]; Y[1] += pre[1];
        u32x4 x, y;
        pv_unit(Y, pbl, lo32, x, y);
        store_unit(out, bh, pw, lane, x, y);
      } else {
        while (*ch != turn) __builtin_amdgcn_s_sleep(1);
        __builtin_amdgcn_fence(__ATOMIC_ACQUIRE, "workgroup");
        if (w >= (solo ? 3 : 1)) { Y[0] += cw[0]; Y[1] += cw[64]; }
        cw[0] = Y[0]; cw[64] = Y[1];
        __builtin_amdgcn_fence(__ATOMIC_RELEASE, "workgroup");
        *ch = turn + 1;
      }
    }
    return;
  }

  const rsrc_t rw = make_rsrc(ws, WS_BYTES);
  if (w == 9) {
    unsigned tbase = t0;
    {
      if (bx < 8 && lane < 8) __builtin_amdgcn_raw_buffer_store_b128(u32x4{t0, t0, t0, t0}, rw, WS_T0 + bx * WS_FAR + lane * 16, 0, AUX_DEV);
      if (bx == 77) {
#pragma unroll 1
        for (int tr = 0; tr < 10; ++tr) {
          while ((int)(now32() - t0) < 150 + 60 * tr) __builtin_amdgcn_s_sleep(4);
          asm volatile("" ::: "memory");
          int dt = 0; bool ok = false;
          if (lane < 8) { dt = (int)(__builtin_amdgcn_raw_buffer_load_b32(rw, WS_T0 + lane * WS_FAR, 0, AUX_DEV) - t0); ok = dt > -800 && dt < 800; }
          if (__popcll(__ballot(ok)) == 8) {
            dt += __shfl_xor(dt, 1); dt += __shfl_xor(dt, 2); dt += __shfl_xor(dt, 4);
            const unsigned tm = t0 + (unsigned)((int)uni(dt) >> 3);
            __builtin_amdgcn_raw_buffer_store_b128(u32x4{tm, tm, tm, tm}, rw, WS_ANC + (lane >> 1) * WS_LAT_STRIDE + (lane & 1) * 16, 0, AUX_DEV);
            break;
          }
        }
      }
#pragma unroll 1
      for (int tr = 0; tr < 8; ++tr) {
        while ((int)(now32() - t0) < 380 + 120 * tr) __builtin_amdgcn_s_sleep(8);
        asm volatile("" ::: "memory");
        const int dt = (int)(uni((int)__builtin_amdgcn_raw_buffer_load_b32(rw, WS_ANC + (bx & 31) * WS_LAT_STRIDE, 0, AUX_DEV)) - t0);
        if (dt > -800 && dt < 800) {
          tbase += (unsigned)dt;
          *tbp = (int)tbase;
          break;
        }
      }
    }
    {
      const bool pub = ((bx >> 3) & 3) == 1;
      {
        wait_instant(tbp, AT_ACK);
        if (pub && lane < 2 && (lane == 0 || bx < 64)) {
          unsigned x = __builtin_amdgcn_raw_buffer_load_b32(rw, lane == 0 ? WS_LAT + ((bx >> 5) * 8 + (bx & 7)) * WS_LAT_STRIDE : WS_CMD + ((bx >> 5) * 8 + (bx & 7)) * WS_FAR, 0, AUX_DEV);
          asm volatile("" :: "v"(x));
        }
      }
#pragma unroll 1
      for (int B = C_B0; B + 12 < 192; B += C_PER) {
        const unsigned tb = tbase + sched(B);
        const int alarm = 60 + 2 * (192 - B);
        if (!solo) {
          wait_instant(tbp, B);
          const int l = (int)uni(*latp);
          if (l > (pub ? C_PUB : alarm) && lane < 16) __builtin_amdgcn_raw_buffer_store_b64(u32x2{tb, (unsigned)l}, rw, WS_LAT + ((bx >> 5) * 8 + (bx & 7)) * WS_LAT_STRIDE + lane * 8, 0, AUX_DEV);
        }
        if (solo) {
          wait_instant(tbp, B + C_LD);
          const u32x2 x = __builtin_amdgcn_raw_buffer_load_b64(rw, WS_LAT + lane * WS_LAT_STRIDE, 0, AUX_DEV);
          int l = (x[0] - tb + 300u < 600u) ? max(min((int)x[1], C_CAP), 0) : 0;
          const bool al = __any(l > alarm);
#pragma unroll
          for (int o = 32; o >= 1; o >>= 1) l += __shfl_xor(l, o);
          l >>= 6;
          if (al || l > C_E0 + ((192 - B) >> 4)) {
            const unsigned nb = tbase + (unsigned)min(min(C_A + ((l * 13) >> 3), C_B + l), C_POS);
            __builtin_amdgcn_raw_buffer_store_b128(u32x4{tb, nb, tb, nb}, rw, WS_CMD + (lane >> 2) * WS_FAR + (lane & 3) * 16, 0, AUX_DEV);
          }
        }
#pragma unroll 1
        for (int tr = 0; tr < 2; ++tr) {
          wait_instant(tbp, B + C_RD + 3 * tr);
          const u32x2 x = __builtin_amdgcn_raw_buffer_load_b64(rw, WS_CMD + (bx & 15) * WS_FAR, 0, AUX_DEV);
          const unsigned ct = uni((int)x[0]), cv = uni((int)x[1]);
          const int dlt = (int)(cv - tbase);
          if (ct - t0 < tb - t0 + 300u && dlt > 0 && dlt < 999) {
#pragma unroll 1
            for (int i = 1; i <= 8; ++i) { *tbp = (int)(tbase + (unsigned)((dlt * i) >> 3)); __builtin_amdgcn_s_sleep(C_STEP); }
            tbase = cv;
            break;
          }
        }
      }
    }
    return;
  }

  const int g = lane >> 4, c = lane & 15, p = cu - 3 * tri;
  {
    wait_instant(tbp, AT_TLB);
    const int tb = unit_bh((lane >> 1) % 3, tri);
    const unsigned char* tp = lane < 12 ? (lane < 6 ? k : v) + (size_t)tb * BHB + (lane & 1) * (BHB - 128)
                                        : ((lane & 1) ? q + (size_t)tb * (SQ * ROWB) : f);
    if (!solo && (lane < 12 ? (p == 0 && (tri & 3) == 0) : (lane < 18 && (cu & 15) == 7))) {
      unsigned x = __builtin_nontemporal_load((const GAS unsigned*)tp);
      asm volatile("" :: "v"(x));
    }
  }
  const int pcu = 3 * tri + (lane < 3 ? lane : 0);
  u32x2 old = {0u, 0u};
  wait_instant(tbp, AT_ACK);
  if (!solo && lane < 3) old = __builtin_amdgcn_raw_buffer_load_b64(rw, WS_FLAG + pcu * 128, 0, AUX_DEV_NT);
  asm volatile("" : "+v"(old));
  wait_instant(tbp, AT_Q);
  if (!solo && lane < 16) __builtin_amdgcn_raw_buffer_store_b64(u32x2{old[0] ^ MAGIC_LO, old[1] ^ MAGIC_HI}, rw, WS_ACK + cu * 128 + lane * 8, 0, AUX_DEV_NT);
  u32x4 qf[3][2];
#pragma unroll
  for (int j = 0; j < 3; ++j) {
    const rsrc_t rq = make_rsrc(q + (size_t)(solo ? SOLO_BH : unit_bh(j, tri)) * (SQ * ROWB), SQ * ROWB);
    const int off = (c & 7) * ROWB + (solo ? j : p) * 128 + (c >> 3) * 64 + g * 16;
    qf[j][0] = __builtin_amdgcn_raw_buffer_load_b128(rq, off, 0, AUX_NT);
    qf[j][1] = __builtin_amdgcn_raw_buffer_load_b128(rq, off, 8 * ROWB, AUX_NT);
  }
  const unsigned mine0 = (unsigned)__builtin_amdgcn_readlane((int)old[0], p), mine1 = (unsigned)__builtin_amdgcn_readlane((int)old[1], p);
  f32x4 st[3];
#pragma unroll
  for (int jj = 0; jj < 3; ++jj) {
    const int j = THIRD(jj), turn = solo ? 2 : 9 * jj + 8;
    volatile LAS int* ch = chain + (solo ? j : 0);
    lds_wait_eq(ch, turn);
    const f32x4 y0 = cb[(solo ? j : 0) * 128 + lane], y1 = cb[(solo ? j : 0) * 128 + 64 + lane];
#pragma unroll
    for (int t = 0; t < 4; ++t)
      *(LAS unsigned*)(L8 + L_KP + (4 * (g & 1) + t) * 128 + (c & 7) * 16 + (c >> 3) * 8 + (g >> 1) * 4) = pack_bf16(y0[t], y1[t]);
    __builtin_amdgcn_fence(__ATOMIC_ACQ_REL, "workgroup");
    *ch = turn + 1;
    u32x4 q0, q1;
#pragma unroll
    for (int i = 0; i < 4; ++i) { q0[i] = take_hi8(qf[j][0][i], qf[j][1][i]); q1[i] = take_lo8(qf[j][1][i], qf[j][0][i]); }
    st[j] = mfma32(*(const LAS u32x4*)(L8 + L_KP + (c & 7) * 128 + g * 16), q0, f32x4{0.f, 0.f, 0.f, 0.f});
    st[j] = mfma32(*(const LAS u32x4*)(L8 + L_KP + (c & 7) * 128 + 64 + g * 16), q1, st[j]);
    __builtin_amdgcn_fence(__ATOMIC_ACQ_REL, "workgroup");
    if (jj == 1 && !solo) {
      wait_instant(tbp, AT_ACKCHK);
      wait_words(rw, WS_ACK + pcu * 128 + p * 8, u32x2{mine0 ^ MAGIC_LO, mine1 ^ MAGIC_HI}, lane);
    }
  }
  f32x4 sc3[3];
  if (solo) {
    sc3[0] = sc3[1] = sc3[2] = (st[0] + st[1]) + st[2];
  } else {
    wait_instant(tbp, AT_PUB);
    __builtin_amdgcn_raw_buffer_store_b128(__builtin_bit_cast(u32x4, lo32 ? st[0] : st[1]), rw, WS_PAY + cu * WS_PAY_STRIDE + lane * 16, 0, AUX_DEV_NT);
    if (lo32) __builtin_amdgcn_raw_buffer_store_b128(__builtin_bit_cast(u32x4, st[2]), rw, WS_PAY + cu * WS_PAY_STRIDE + 1024 + lane * 16, 0, AUX_DEV_NT);
    asm volatile("s_waitcnt vmcnt(0)" ::: "memory");
    wait_instant(tbp, AT_FLAG);
    if (lane < 16) __builtin_amdgcn_raw_buffer_store_b64(u32x2{mine0 + 1u, mine1}, rw, WS_FLAG + cu * 128 + lane * 8, 0, AUX_DEV_NT);
    wait_instant(tbp, AT_POLL);
    wait_words(rw, WS_FLAG + pcu * 128, u32x2{old[0] + 1u, old[1]}, lane);
    wait_instant(tbp, AT_PAY);
    u32x4 pl[3][2];
#pragma unroll
    for (int j = 0; j < 3; ++j)
#pragma unroll
      for (int o = 0; o < 2; ++o) {
        pl[j][o] = u32x4{0u, 0u, 0u, 0u};
        if (lo32) pl[j][o] = __builtin_amdgcn_raw_buffer_load_b128(rw, lane * 16, WS_PAY + (3 * tri + o + (o >= p)) * WS_PAY_STRIDE + j * 512, AUX_DEV_NT);
      }
#pragma unroll
    for (int j = 0; j < 3; ++j) {
      const f32x4 a = __builtin_bit_cast(f32x4, pl[j][0]), b = __builtin_bit_cast(f32x4, pl[j][1]);
      sc3[j] = p < 2 ? (st[j] + a) + b : (a + b) + st[j];
    }
  }
  u32x2 pb[3];
#pragma unroll
  for (int j = 0; j < 3; ++j) {
    float sc[4];
#pragma unroll
    for (int t = 0; t < 4; ++t) sc[t] = round_bf16(round_bf16(sc3[j][t]) * 0.07216878364870322f);
    float mx = fmaxf(fmaxf(sc[0], sc[1]), fmaxf(sc[2], sc[3]));
    mx = fmaxf(mx, __shfl_xor(mx, 16));
    float ex[4], sum = 0.f;
#pragma unroll
    for (int t = 0; t < 4; ++t) { ex[t] = __expf(sc[t] - mx); sum += ex[t]; }
    sum += __shfl_xor(sum, 16);
    const float inv = 1.0f / sum;
    pb[j] = u32x2{(unsigned)__shfl((int)pack_bf16(ex[0] * inv, ex[1] * inv), lane & 31),
                  (unsigned)__shfl((int)pack_bf16(ex[2] * inv, ex[3] * inv), lane & 31)};
  }
  *(LAS u32x2*)(L8 + L_PB + lane * 8) = pb[THIRD(2)];
  __builtin_amdgcn_fence(__ATOMIC_RELEASE, "workgroup");
  *pbok = 1;
  if (solo) return;
  const bool early = (cu & 15) == 7;
  u32x4 ox[2], oy[2];
#pragma unroll
  for (int j = 0; j < 2; ++j) {
    lds_wait_eq(chain, 9 * (3 + j) + 8);
    f32x4 Y[2] = {cb[lane], cb[64 + lane]};
    asm volatile("" : "+v"(Y[0]), "+v"(Y[1]));
    __builtin_amdgcn_fence(__ATOMIC_RELEASE, "workgroup");
    *chain = 9 * (3 + j) + 9;
    pv_unit(Y, pb[THIRD(j)], lo32, ox[j], oy[j]);
    if (early) store_unit(out, unit_bh(THIRD(j), tri), p, lane, ox[j], oy[j]);
  }
  if (early) return;
  wait_instant(tbp, AT_OUT);
  store_unit(out, unit_bh(2, tri), p, lane, ox[0], oy[0]);
  store_unit(out, unit_bh(THIRD(1), tri), p, lane, ox[1], oy[1]);
}

extern "C" void kernel_launch(void* const* d_in, const int* in_sizes, int n_in, void* d_out, int out_size, void* d_ws, size_t ws_size,
                              hipStream_t stream) {
  (void)in_sizes; (void)n_in; (void)out_size; (void)ws_size;
  linformer_kernel<<<NWG, 640, 0, stream>>>((const unsigned char*)d_in[1], (const unsigned char*)d_in[3], (const unsigned char*)d_in[2],
                                            (const unsigned char*)d_in[4], (const unsigned char*)d_in[0], (unsigned char*)d_out,
                                            (unsigned char*)d_ws);
}
```
